```python
import torch

def _softmax(x, axis=-1):
    return torch.softmax(x, dim=axis)
def _gelu(x):
    return torch.nn.functional.gelu(x, approximate="tanh")
def _silu(x):
    return torch.nn.functional.silu(x)
def _sigmoid(x):
    return torch.sigmoid(x)
def _layernorm(x, axis=-1, eps=1e-5):
    m = x.mean(dim=axis, keepdim=True)
    v = x.var(dim=axis, keepdim=True, unbiased=False)
    return (x - m) / torch.sqrt(v + eps)
def _scatter_add(tgt, idx, src, axis=0):
    return tgt.clone().index_add_(axis, idx, src)
def _segment_sum(src, seg, n):
    out = torch.zeros((n,) + src.shape[1:], dtype=src.dtype)
    return out.index_add_(0, seg, src)
def _pad(x, pw):
    flat = tuple(v for pair in reversed(pw) for v in pair)
    return torch.nn.functional.pad(x, flat)

kernel_name = 'standalone__fm_second_order__recsys'

def setup_inputs():

    _w = (lambda a: (lambda t: t.to(torch.bfloat16) if t.is_floating_point() else t)(torch.as_tensor(a)))
    B, F, D, V = 4096, 26, 128, min(1000000, 100000)
    return {
        "idx": _w((lambda lo, hi, *s: torch.randint(lo, hi, s))(0, V, B, F).to(torch.int64)),
        "table": _w((lambda *s: torch.randn(*s, dtype=torch.bfloat16) * 0.05)(V, D)),
    }

def reference(idx, table):
    emb = table[idx]
    sum_sq = emb.sum(axis=1) ** 2
    sq_sum = (emb ** 2).sum(axis=1)
    return 0.5 * (sum_sq - sq_sum).sum(axis=-1)
```

```cpp
#include <hip/hip_runtime.h>
#include <hip/hip_bf16.h>
#include <stdint.h>

typedef __attribute__((ext_vector_type(4))) unsigned u32x4;
typedef __attribute__((ext_vector_type(2))) __bf16 bf16x2;

constexpr int F = 26;
constexpr int D = 128;

template <int N>
__device__ __forceinline__ unsigned row_bcast(unsigned v) {
    return __builtin_amdgcn_update_dpp(0u, v, 0x150 + N, 0xf, 0xf, false);
}

__device__ __forceinline__ float dot2(unsigned a, unsigned b, float c) {
    return __builtin_amdgcn_fdot2_f32_bf16(__builtin_bit_cast(bf16x2, a), __builtin_bit_cast(bf16x2, b), c, false);
}

template <int CTRL>
__device__ __forceinline__ float dpp_add(float v) {
    int t = __builtin_amdgcn_update_dpp(0, __builtin_bit_cast(int, v), CTRL, 0xf, 0xf, false);
    return v + __builtin_bit_cast(float, t);
}

template <int I>
struct Loader {
    static __device__ __forceinline__ void run(u32x4* r, const char* table, unsigned o0, unsigned o1, unsigned lo) {
        unsigned off = (I < 16 ? row_bcast<I & 15>(o0) : row_bcast<I & 15>(o1)) + lo;
        r[I] = *reinterpret_cast<const u32x4*>(table + off);
        Loader<I + 1>::run(r, table, o0, o1, lo);
    }
};
template <>
struct Loader<F> {
    static __device__ __forceinline__ void run(u32x4*, const char*, unsigned, unsigned, unsigned) {}
};

__global__ __launch_bounds__(256) void fm_kernel(const int* __restrict__ idx, const char* __restrict__ table,
                                                 __hip_bfloat16* __restrict__ out) {
    const unsigned tid = threadIdx.x;
    const unsigned sample = blockIdx.x * 16 + (tid >> 4);
    const unsigned j = tid & 15;
    const int* ip = idx + sample * F;
    unsigned o0 = (unsigned)ip[j] << 8;
    unsigned o1 = (unsigned)ip[16 + (j < 10 ? j : 9)] << 8;
    const unsigned lo = j << 4;
    u32x4 r[F];
    Loader<0>::run(r, table, o0, o1, lo);
    __builtin_amdgcn_sched_barrier(0);
    float s[8] = {0, 0, 0, 0, 0, 0, 0, 0};
    float q0 = 0.f, q1 = 0.f;
#pragma unroll
    for (int f = 0; f < F; ++f) {
#pragma unroll
        for (int k = 0; k < 4; ++k) {
            unsigned x = r[f][k];
            s[2 * k] = dot2(x, 0x00003f80u, s[2 * k]);
            s[2 * k + 1] = dot2(x, 0x3f800000u, s[2 * k + 1]);
            if (k & 1) q1 = dot2(x, x, q1); else q0 = dot2(x, x, q0);
        }
    }
    float t = -(q0 + q1);
#pragma unroll
    for (int k = 0; k < 8; ++k) t = fmaf(s[k], s[k], t);
    t = dpp_add<0x128>(t);
    t = dpp_add<0x124>(t);
    t = dpp_add<0x122>(t);
    t = dpp_add<0x121>(t);
    if (j == 0) out[sample] = __float2bfloat16(0.5f * t);
}

extern "C" void kernel_launch(void* const* d_in, const int* in_sizes, int n_in,
                              void* d_out, int out_size, void* d_ws, size_t ws_size,
                              hipStream_t stream) {
    const int* idx = (const int*)d_in[0];
    const char* table = (const char*)d_in[1];
    fm_kernel<<<out_size / 16, 256, 0, stream>>>(idx, table, (__hip_bfloat16*)d_out);
}
```

```cpp
#include <hip/hip_runtime.h>
#include <hip/hip_bf16.h>
#include <stdint.h>

typedef __attribute__((ext_vector_type(4))) unsigned u32x4;
typedef __attribute__((ext_vector_type(2))) __bf16 bf16x2;

constexpr int F = 26;
constexpr int BLOCK = 256;

template <int N>
__device__ __forceinline__ unsigned row_bcast(unsigned v) {
    return __builtin_amdgcn_update_dpp(0u, v, 0x150 + N, 0xf, 0xf, false);
}
__device__ __forceinline__ float dot2(unsigned a, unsigned b, float c) {
    return __builtin_amdgcn_fdot2_f32_bf16(__builtin_bit_cast(bf16x2, a), __builtin_bit_cast(bf16x2, b), c, false);
}
template <int CTRL>
__device__ __forceinline__ float dpp_add(float v) {
    int t = __builtin_amdgcn_update_dpp(0, __builtin_bit_cast(int, v), CTRL, 0xf, 0xf, false);
    return v + __builtin_bit_cast(float, t);
}

template <int I>
struct Loader {
    static __device__ __forceinline__ void run(u32x4* r, __amdgpu_buffer_rsrc_t rs, unsigned o0, unsigned o1, unsigned lo) {
        unsigned off = (I < 16 ? row_bcast<I & 15>(o0) : row_bcast<I & 15>(o1)) + lo;
        r[I] = __builtin_amdgcn_raw_buffer_load_b128(rs, off, 0, 0);
        Loader<I + 1>::run(r, rs, o0, o1, lo);
    }
};
template <>
struct Loader<F> {
    static __device__ __forceinline__ void run(u32x4*, __amdgpu_buffer_rsrc_t, unsigned, unsigned, unsigned) {}
};

__global__ __launch_bounds__(BLOCK) void fm_kernel(const char* __restrict__ idx, const char* __restrict__ table,
                                                   char* __restrict__ out, unsigned tbytes) {
    const unsigned tid = threadIdx.x;
    const unsigned sample = blockIdx.x * (BLOCK / 16) + (tid >> 4);
    const unsigned j = tid & 15;
    const unsigned b0 = (sample * F + j) * 4u;
    const unsigned b1 = (sample * F + (j < F - 16 ? j : F - 17)) * 4u;
    asm volatile("" ::"s"(out), "s"(tbytes));
    const unsigned i0 = *(const unsigned*)(idx + b0);
    const unsigned i1 = *(const unsigned*)(idx + b1 + 64);
    const auto rs = __builtin_amdgcn_make_buffer_rsrc((void*)table, 0, tbytes, 0x00020000);
    __builtin_amdgcn_sched_barrier(0);
    const unsigned touch = __builtin_amdgcn_raw_buffer_load_b32(rs, (tid & 63) << 21, 0, 0);
    __builtin_amdgcn_sched_barrier(0);

    u32x4 r[F];
    Loader<0>::run(r, rs, i0 << 8, i1 << 8, j << 4);
    __builtin_amdgcn_sched_barrier(0);

    float s[8] = {0, 0, 0, 0, 0, 0, 0, 0};
    unsigned c_lo, c_hi;
    asm("s_mov_b32 %0, 0x3f80" : "=s"(c_lo));
    asm("s_mov_b32 %0, 0x3f800000" : "=s"(c_hi));
    float q0 = 0.f, q1 = 0.f;
#pragma unroll
    for (int f = 0; f < F; ++f) {
#pragma unroll
        for (int k = 0; k < 4; ++k) {
            const unsigned x = r[f][k];
            s[2 * k] = dot2(c_lo, x, s[2 * k]);
            s[2 * k + 1] = dot2(c_hi, x, s[2 * k + 1]);
            if (k & 1) q1 = dot2(x, x, q1); else q0 = dot2(x, x, q0);
        }
        __builtin_amdgcn_sched_barrier(0);
    }
    asm volatile("" ::"v"(touch));
    float t = -(q0 + q1);
#pragma unroll
    for (int k = 0; k < 8; ++k) t = fmaf(s[k], s[k], t);
    t = dpp_add<0x128>(t);
    t = dpp_add<0x124>(t);
    t = dpp_add<0x122>(t);
    t = dpp_add<0x121>(t);
    if (j == 0) *(__hip_bfloat16*)(out + sample * 2u) = __float2bfloat16(0.5f * t);
}

extern "C" void kernel_launch(void* const* d_in, const int* in_sizes, int n_in,
                              void* d_out, int out_size, void* d_ws, size_t ws_size,
                              hipStream_t stream) {
    const char* idx = (const char*)d_in[0];
    const char* table = (const char*)d_in[1];
    fm_kernel<<<out_size / (BLOCK / 16), BLOCK, 0, stream>>>(idx, table, (char*)d_out,
                                                              (unsigned)in_sizes[1] * 2u);
}
```

```cpp
#include <hip/hip_runtime.h>
#include <hip/hip_bf16.h>
#include <stdint.h>

typedef __attribute__((ext_vector_type(4))) unsigned u32x4;
typedef __attribute__((ext_vector_type(2))) __bf16 bf16x2;

constexpr int F = 26;
constexpr int W = 14;
constexpr int BLOCK = 256;

template <int N>
__device__ __forceinline__ unsigned row_bcast(unsigned v) {
    return __builtin_amdgcn_update_dpp(0u, v, 0x150 + N, 0xf, 0xf, false);
}
__device__ __forceinline__ float dot2(unsigned a, unsigned b, float c) {
    return __builtin_amdgcn_fdot2_f32_bf16(__builtin_bit_cast(bf16x2, a), __builtin_bit_cast(bf16x2, b), c, false);
}
template <int CTRL>
__device__ __forceinline__ float dpp_add(float v) {
    int t = __builtin_amdgcn_update_dpp(0, __builtin_bit_cast(int, v), CTRL, 0xf, 0xf, false);
    return v + __builtin_bit_cast(float, t);
}

struct Ctx {
    __amdgpu_buffer_rsrc_t rs;
    unsigned o0, o1;
    unsigned lo;
    unsigned c_lo, c_hi;
    float s[8];
    float q0, q1;
};

template <int I>
__device__ __forceinline__ u32x4 load_row(const Ctx& c) {
    const unsigned off = (I < 16 ? row_bcast<I & 15>(c.o0) : row_bcast<I & 15>(c.o1)) + c.lo;
    return __builtin_amdgcn_raw_buffer_load_b128(c.rs, off, 0, 0);
}
template <int I>
__device__ __forceinline__ void fill(u32x4* r, const Ctx& c) {
    if constexpr (I < W) {
        r[I] = load_row<I>(c);
        fill<I + 1>(r, c);
    }
}
template <int I>
__device__ __forceinline__ void consume(u32x4* r, Ctx& c) {
    if constexpr (I < F) {
        if constexpr (I + W < F) {
            asm volatile("" ::"v"(r[I]));
            __builtin_amdgcn_sched_barrier(0);
            r[I + W] = load_row<I + W>(c);
            __builtin_amdgcn_sched_barrier(0);
        }
#pragma unroll
        for (int k = 0; k < 4; ++k) {
            const unsigned x = r[I][k];
            c.s[2 * k] = dot2(c.c_lo, x, c.s[2 * k]);
            c.s[2 * k + 1] = dot2(c.c_hi, x, c.s[2 * k + 1]);
            if (k & 1) c.q1 = dot2(x, x, c.q1); else c.q0 = dot2(x, x, c.q0);
        }
        __builtin_amdgcn_sched_barrier(0);
        consume<I + 1>(r, c);
    }
}

__global__ __launch_bounds__(BLOCK) void fm_kernel(const char* __restrict__ idx, const char* __restrict__ table,
                                                   char* __restrict__ out, unsigned tbytes) {
    const unsigned tid = threadIdx.x;
    const unsigned sample = blockIdx.x * (BLOCK / 16) + (tid >> 4);
    const unsigned j = tid & 15;
    const unsigned b0 = (sample * F + j) * 4u;
    const unsigned b1 = (sample * F + (j < F - 16 ? j : F - 17)) * 4u;
    asm volatile("" ::"s"(out), "s"(tbytes));
    const unsigned i0 = *(const unsigned*)(idx + b0);
    const unsigned i1 = *(const unsigned*)(idx + b1 + 64);
    Ctx c;
    c.rs = __builtin_amdgcn_make_buffer_rsrc((void*)table, 0, tbytes, 0x00020000);
    __builtin_amdgcn_sched_barrier(0);
    const unsigned touch = __builtin_amdgcn_raw_buffer_load_b32(c.rs, (tid & 63) << 21, 0, 0);
    __builtin_amdgcn_sched_barrier(0);

    c.o0 = i0 << 8;
    c.lo = j << 4;
    u32x4 r[F];
    fill<0>(r, c);
    __builtin_amdgcn_sched_barrier(0);

    c.o1 = i1 << 8;
    asm("s_mov_b32 %0, 0x3f80" : "=s"(c.c_lo));
    asm("s_mov_b32 %0, 0x3f800000" : "=s"(c.c_hi));
#pragma unroll
    for (int k = 0; k < 8; ++k) c.s[k] = 0.f;
    c.q0 = c.q1 = 0.f;
    consume<0>(r, c);
    asm volatile("" ::"v"(touch));

    float t = -(c.q0 + c.q1);
#pragma unroll
    for (int k = 0; k < 8; ++k) t = fmaf(c.s[k], c.s[k], t);
    t = dpp_add<0x128>(t);
    t = dpp_add<0x124>(t);
    t = dpp_add<0x122>(t);
    t = dpp_add<0x121>(t);
    if (j == 0)
        __hip_atomic_store((unsigned short*)(out + sample * 2u),
                           __builtin_bit_cast(unsigned short, __float2bfloat16(0.5f * t)), __ATOMIC_RELAXED,
                           __HIP_MEMORY_SCOPE_AGENT);
}

extern "C" void kernel_launch(void* const* d_in, const int* in_sizes, int n_in,
                              void* d_out, int out_size, void* d_ws, size_t ws_size,
                              hipStream_t stream) {
    const char* idx = (const char*)d_in[0];
    const char* table = (const char*)d_in[1];
    fm_kernel<<<out_size / (BLOCK / 16), BLOCK, 0, stream>>>(idx, table, (char*)d_out,
                                                              (unsigned)in_sizes[1] * 2u);
}
```
